```python
import torch

def _softmax(x, axis=-1):
    return torch.softmax(x, dim=axis)
def _gelu(x):
    return torch.nn.functional.gelu(x, approximate="tanh")
def _silu(x):
    return torch.nn.functional.silu(x)
def _sigmoid(x):
    return torch.sigmoid(x)
def _layernorm(x, axis=-1, eps=1e-5):
    m = x.mean(dim=axis, keepdim=True)
    v = x.var(dim=axis, keepdim=True, unbiased=False)
    return (x - m) / torch.sqrt(v + eps)
def _scatter_add(tgt, idx, src, axis=0):
    return tgt.clone().index_add_(axis, idx, src)
def _segment_sum(src, seg, n):
    out = torch.zeros((n,) + src.shape[1:], dtype=src.dtype)
    return out.index_add_(0, seg, src)
def _pad(x, pw):
    flat = tuple(v for pair in reversed(pw) for v in pair)
    return torch.nn.functional.pad(x, flat)

kernel_name = 'standalone__gqa_softmax__decode'

def setup_inputs():

    _w = (lambda a: (lambda t: t.to(torch.bfloat16) if t.is_floating_point() else t)(torch.as_tensor(a)))
    B, Hq, Hkv, Sq, Sk, D = 1, 32, 32, 1, 32768, 128
    return {
        "q": _w((lambda *s: torch.randn(*s, dtype=torch.bfloat16) * 0.8)(B, Hq, Sq, D)),
        "k": _w((lambda *s: torch.randn(*s, dtype=torch.bfloat16) * 0.8)(B, Hkv, Sk, D)),
        "v": _w((lambda *s: torch.randn(*s, dtype=torch.bfloat16) * 0.05)(B, Hkv, Sk, D)),
    }

def reference(q, k, v):
    B, Hq, Sq, D = q.shape
    Hkv = k.shape[1]
    rep = Hq // Hkv
    k = (lambda x, n, axis: torch.repeat_interleave(x, n, dim=axis))(k, rep, axis=1)
    v = (lambda x, n, axis: torch.repeat_interleave(x, n, dim=axis))(v, rep, axis=1)
    scale = D ** -0.5
    scores = torch.einsum("bhqd,bhkd->bhqk", q, k) * scale
    p = _softmax(scores, axis=-1)
    return torch.einsum("bhqk,bhkd->bhqd", p, v)
```

```cpp
#include <hip/hip_runtime.h>
#include <hip/hip_bf16.h>
#include <stdint.h>

using bf16x2 = __attribute__((ext_vector_type(2))) __bf16;
using u32x4  = __attribute__((ext_vector_type(4))) unsigned;
using f32x4  = __attribute__((ext_vector_type(4))) float;

constexpr int H = 32, SK = 32768, D = 128;
constexpr int NSPLIT = 8;
constexpr int NW = 8;
constexpr int T = 8;
constexpr int TILE_KEYS = 4 * T;
constexpr int KEYS_PER_WG = SK / NSPLIT;
constexpr int TILES_PER_WAVE = KEYS_PER_WG / (NW * TILE_KEYS);

__device__ __forceinline__ float dot2(unsigned k, unsigned q, float acc) {
  return __builtin_amdgcn_fdot2_f32_bf16(__builtin_bit_cast(bf16x2, k), __builtin_bit_cast(bf16x2, q), acc, false);
}
template <int CTRL> __device__ __forceinline__ float dpp(float x) {
  return __builtin_bit_cast(float, __builtin_amdgcn_mov_dpp(__builtin_bit_cast(int, x), CTRL, 0xf, 0xf, true));
}
constexpr int XOR1 = 0xB1, XOR2 = 0x4E, XOR7 = 0x141, XOR8 = 0x128, ROR4 = 0x124;
__device__ __forceinline__ float xrow16_max(float x) {
  auto s = __builtin_amdgcn_permlane16_swap(__float_as_uint(x), __float_as_uint(x), false, false);
  x = fmaxf(__uint_as_float(s[0]), __uint_as_float(s[1]));
  auto t = __builtin_amdgcn_permlane32_swap(__float_as_uint(x), __float_as_uint(x), false, false);
  return fmaxf(__uint_as_float(t[0]), __uint_as_float(t[1]));
}
__device__ __forceinline__ float xrow16_sum(float x) {
  auto s = __builtin_amdgcn_permlane16_swap(__float_as_uint(x), __float_as_uint(x), false, false);
  x = __uint_as_float(s[0]) + __uint_as_float(s[1]);
  auto t = __builtin_amdgcn_permlane32_swap(__float_as_uint(x), __float_as_uint(x), false, false);
  return __uint_as_float(t[0]) + __uint_as_float(t[1]);
}
__device__ __forceinline__ float row16_max(float x) {
  x = fmaxf(x, dpp<XOR1>(x)); x = fmaxf(x, dpp<XOR2>(x)); x = fmaxf(x, dpp<XOR7>(x)); x = fmaxf(x, dpp<XOR8>(x)); return x;
}
__device__ __forceinline__ float row16_sum(float x) {
  x += dpp<XOR1>(x); x += dpp<XOR2>(x); x += dpp<XOR7>(x); x += dpp<XOR8>(x); return x;
}

__global__ void __launch_bounds__(NW * 64) attn_split(const u32x4* __restrict__ q, const u32x4* __restrict__ k,
                                                      const u32x4* __restrict__ v, float* __restrict__ ws) {
  __shared__ __attribute__((aligned(16))) float lds[NW * 64 + NW * (D + 4)];
  const int h = blockIdx.x / NSPLIT, s = blockIdx.x % NSPLIT;
  const int tid = threadIdx.x, lane = tid & 63, wave = __builtin_amdgcn_readfirstlane(tid >> 6);
  const int r = lane >> 4, c = lane & 15;
  constexpr float C2 = 0.088388347648318440f * 1.4426950408889634f;
  const size_t base = (size_t)h * SK * 16 + (size_t)(s * NW + wave) * (TILE_KEYS * 16) + lane;
  constexpr size_t STEP = (size_t)NSPLIT * NW * TILE_KEYS * 16;
  const u32x4* kp = k + base;
  const u32x4* vp = v + base;
  u32x4 Kn[T], Vn[T];
#pragma unroll
  for (int t = 0; t < T; ++t) Kn[t] = __builtin_nontemporal_load(kp + t * 64);
#pragma unroll
  for (int t = 0; t < T; ++t) Vn[t] = __builtin_nontemporal_load(vp + t * 64);
  const u32x4 qv = q[h * 16 + c];
  float m = -INFINITY, l = 0.f, acc[8];
#pragma unroll
  for (int j = 0; j < 8; ++j) acc[j] = 0.f;
  float* prow = lds + wave * 64 + r * 16;
  for (int i = 0; i < TILES_PER_WAVE; ++i) {
    const bool more = (i + 1 < TILES_PER_WAVE);
    kp += STEP; vp += STEP;
    float x[T];
#pragma unroll
    for (int t = 0; t < T; ++t) {
      float d = dot2(Kn[t][0], qv[0], 0.f); d = dot2(Kn[t][1], qv[1], d); d = dot2(Kn[t][2], qv[2], d); d = dot2(Kn[t][3], qv[3], d);
      x[t] = d;
      if (more) Kn[t] = __builtin_nontemporal_load(kp + t * 64);
    }
#define TR_STEP(HALF, CTRL, BIT) _Pragma("unroll") for (int hh = 0; hh < (HALF); ++hh) { \
      const float lo_ = x[hh], hi_ = x[hh + (HALF)]; const float t1_ = lo_ + dpp<CTRL>(lo_), t2_ = hi_ + dpp<CTRL>(hi_); \
      x[hh] = (c & (BIT)) ? t2_ : t1_; }
    TR_STEP(4, XOR7, 4) TR_STEP(2, XOR2, 2) TR_STEP(1, XOR1, 1) x[0] += dpp<XOR8>(x[0]);
#undef TR_STEP
    const float sc = x[0] * C2;
    float pm = xrow16_max(row16_max(sc));
    const float mn = fmaxf(m, pm);
    const float alpha = __builtin_amdgcn_exp2f(m - mn);
    const float p = __builtin_amdgcn_exp2f(sc - mn);
    l = fmaf(l, alpha, (c & 8) ? 0.f : p); m = mn;
#pragma unroll
    for (int j = 0; j < 8; ++j) acc[j] *= alpha;
    prow[c] = p;
    asm volatile("s_waitcnt lgkmcnt(0)" ::: "memory");
    f32x4 pr[2];
#pragma unroll
    for (int u = 0; u < 2; ++u) pr[u] = *reinterpret_cast<const f32x4*>(prow + 4 * u);
#pragma unroll
    for (int t = 0; t < T; ++t) {
      const float pt = pr[t >> 2][t & 3];
#pragma unroll
      for (int j = 0; j < 4; ++j) {
        acc[2 * j]     = fmaf(pt, __builtin_bit_cast(float, Vn[t][j] << 16), acc[2 * j]);
        acc[2 * j + 1] = fmaf(pt, __builtin_bit_cast(float, Vn[t][j] & 0xffff0000u), acc[2 * j + 1]);
      }
      if (more) Vn[t] = __builtin_nontemporal_load(vp + t * 64);
    }
  }
#pragma unroll
  for (int j = 0; j < 8; ++j) acc[j] = xrow16_sum(acc[j]);
  l = xrow16_sum(row16_sum(l));
  float* rec = lds + NW * 64 + wave * (D + 4);
  if (r == 0) {
    *reinterpret_cast<f32x4*>(rec + 4 + 8 * c) = f32x4{acc[0], acc[1], acc[2], acc[3]};
    *reinterpret_cast<f32x4*>(rec + 4 + 8 * c + 4) = f32x4{acc[4], acc[5], acc[6], acc[7]};
    if (c == 0) { rec[0] = m; rec[1] = l; }
  }
  __syncthreads();
  if (tid < D) {
    const float* mrg = lds + NW * 64;
    float M = -INFINITY;
#pragma unroll
    for (int w = 0; w < NW; ++w) M = fmaxf(M, mrg[w * (D + 4)]);
    float L = 0.f, o = 0.f;
#pragma unroll
    for (int w = 0; w < NW; ++w) {
      const float wt = __builtin_amdgcn_exp2f(mrg[w * (D + 4)] - M);
      L = fmaf(wt, mrg[w * (D + 4) + 1], L); o = fmaf(wt, mrg[w * (D + 4) + 4 + tid], o);
    }
    float* out = ws + (size_t)blockIdx.x * (D + 4);
    out[4 + tid] = o;
    if (tid == 0) { out[0] = M; out[1] = L; }
  }
}

__global__ void __launch_bounds__(128) attn_combine(const float* __restrict__ ws, __hip_bfloat16* __restrict__ out) {
  const int h = blockIdx.x, tid = threadIdx.x;
  const float* p = ws + (size_t)h * NSPLIT * (D + 4);
  float M = -INFINITY;
#pragma unroll
  for (int s = 0; s < NSPLIT; ++s) M = fmaxf(M, p[s * (D + 4)]);
  float L = 0.f, o = 0.f;
#pragma unroll
  for (int s = 0; s < NSPLIT; ++s) {
    const float wt = __builtin_amdgcn_exp2f(p[s * (D + 4)] - M);
    L = fmaf(wt, p[s * (D + 4) + 1], L); o = fmaf(wt, p[s * (D + 4) + 4 + tid], o);
  }
  out[h * D + tid] = __float2bfloat16(o / L);
}

extern "C" void kernel_launch(void* const* d_in, const int* in_sizes, int n_in,
                              void* d_out, int out_size, void* d_ws, size_t ws_size,
                              hipStream_t stream) {
  attn_split<<<H * NSPLIT, NW * 64, 0, stream>>>((const u32x4*)d_in[0], (const u32x4*)d_in[1], (const u32x4*)d_in[2], (float*)d_ws);
  attn_combine<<<H, D, 0, stream>>>((const float*)d_ws, (__hip_bfloat16*)d_out);
}
```

```cpp
#include <hip/hip_runtime.h>
#include <stdint.h>

__global__ void __launch_bounds__(64, 1) attn_decode(const void* q, const void* k, const void* v, void* out, void* ws) {
  const uint64_t ka = (uint64_t)__builtin_amdgcn_kernarg_segment_ptr();
  asm volatile(
      "s_memrealtime s[36:37]\n\t"
      "s_load_dwordx4 s[68:71], %[ka], 0x0\n\t"
      "s_load_dwordx4 s[72:75], %[ka], 0x10\n\t"
      "s_load_dwordx2 s[76:77], %[ka], 0x20\n\t"
      "s_cmp_ge_u32 %[bid], 16\n\t"
      "s_cbranch_scc1 206f\n\t"
      "s_getpc_b64 s[48:49]\n\t"
      "205:\n\t"
      "s_add_u32 s48, s48, 207f-205b\n\t"
      "s_addc_u32 s49, s49, 0\n\t"
      "s_and_b32 s49, s49, 0xffff\n\t"
      "s_mov_b32 s50, 109f-207f\n\t"
      "s_mov_b32 s51, 0x20000\n\t"
      "v_lshlrev_b32 v57, 4, %[lane]\n\t"
      "v_add_u32 v59, 4096, v57\n\t"
      "buffer_load_dwordx4 v[162:165], v57, s[48:51], 0 offen offset:0\n\t"
      "buffer_load_dwordx4 v[162:165], v57, s[48:51], 0 offen offset:1024\n\t"
      "buffer_load_dwordx4 v[162:165], v57, s[48:51], 0 offen offset:2048\n\t"
      "buffer_load_dwordx4 v[162:165], v57, s[48:51], 0 offen offset:3072\n\t"
      "buffer_load_dwordx4 v[162:165], v59, s[48:51], 0 offen offset:0\n\t"
      "buffer_load_dwordx4 v[162:165], v59, s[48:51], 0 offen offset:1024\n\t"
      "206:\n\t"
      "s_lshr_b32 s58, %[bid], 3\n\t"
      "s_and_b32 s63, %[bid], 7\n\t"
      "s_mov_b32 s80, s63\n\t"
      "s_and_b32 s59, s58, 7\n\t"
      "s_bcnt1_i32_b32 s59, s59\n\t"
      "s_and_b32 s59, s59, 1\n\t"
      "s_xor_b32 s80, s80, s59\n\t"
      "s_and_b32 s59, s58, 13\n\t"
      "s_bcnt1_i32_b32 s59, s59\n\t"
      "s_and_b32 s59, s59, 1\n\t"
      "s_lshl_b32 s59, s59, 1\n\t"
      "s_xor_b32 s80, s80, s59\n\t"
      "s_and_b32 s59, s58, 25\n\t"
      "s_bcnt1_i32_b32 s59, s59\n\t"
      "s_and_b32 s59, s59, 1\n\t"
      "s_lshl_b32 s59, s59, 2\n\t"
      "s_xor_b32 s80, s80, s59\n\t"
      "s_lshl_b32 s80, s80, 20\n\t"
      "s_mov_b32 s52, s80\n\t"
      "s_lshl_b32 s60, s58, 23\n\t"
      "s_mov_b32 s42, 0x800000\n\t"
      "s_mov_b32 s43, 0x20000\n\t"
      "v_and_b32 v52, 3, %[lane]\n\t"
      "v_lshrrev_b32 v53, 4, %[lane]\n\t"
      "v_lshlrev_b32 v4, 4, v52\n\t"
      "v_lshl_add_u32 v4, v53, 6, v4\n\t"
      "v_bfe_u32 v10, %[lane], 2, 2\n\t"
      "v_lshl_add_u32 v62, v10, 8, v4\n\t"
      "v_add_u32 v63, 1024, v62\n\t"
      "v_add_u32 v64, 2048, v62\n\t"
      "v_add_u32 v65, 3072, v62\n\t"
      "v_lshl_add_u32 v16, v10, 12, v4\n\t"
      "v_xor_b32 v17, 1, v10\n\t"
      "v_lshl_add_u32 v17, v17, 12, v4\n\t"
      "v_xor_b32 v18, 2, v10\n\t"
      "v_lshl_add_u32 v18, v18, 12, v4\n\t"
      "v_xor_b32 v19, 3, v10\n\t"
      "v_lshl_add_u32 v19, v19, 12, v4\n\t"
      "s_movk_i32 s50, 0x100\n\t"
      "s_mov_b32 s51, 0x20000\n\t"
      "s_movk_i32 s66, 0x200\n\t"
      "s_mov_b32 s67, 0x20000\n\t"
      "s_lshl_b32 s61, s58, 8\n\t"
      "s_mul_i32 s62, s58, 4352\n\t"
      "s_waitcnt lgkmcnt(0)\n\t"
      "s_add_u32 s40, s70, s60\n\t"
      "s_addc_u32 s41, s71, 0\n\t"
      "s_and_b32 s41, s41, 0xffff\n\t"
      "buffer_load_dwordx4 v[66:69], v62, s[40:43], s52 offen nt\n\t"
      "buffer_load_dwordx4 v[70:73], v63, s[40:43], s52 offen nt\n\t"
      "buffer_load_dwordx4 v[74:77], v64, s[40:43], s52 offen nt\n\t"
      "buffer_load_dwordx4 v[78:81], v65, s[40:43], s52 offen nt\n\t"
      "s_add_u32 s48, s68, s61\n\t"
      "s_addc_u32 s49, s69, 0\n\t"
      "s_and_b32 s49, s49, 0xffff\n\t"
      "buffer_load_dwordx4 v[20:23], v16, s[48:51], 0 offen\n\t"
      "buffer_load_dwordx4 v[24:27], v17, s[48:51], 0 offen\n\t"
      "buffer_load_dwordx4 v[28:31], v18, s[48:51], 0 offen\n\t"
      "buffer_load_dwordx4 v[32:35], v19, s[48:51], 0 offen\n\t"
      "s_add_u32 s64, s76, s62\n\t"
      "s_addc_u32 s65, s77, 0\n\t"
      "s_and_b32 s65, s65, 0xffff\n\t"
      "buffer_load_dword v14, off, s[64:67], 0 sc1\n\t"
      "s_bitset1_b32 s52, 15\n\t"
      "buffer_load_dwordx4 v[82:85], v62, s[40:43], s52 offen nt\n\t"
      "buffer_load_dwordx4 v[86:89], v63, s[40:43], s52 offen nt\n\t"
      "buffer_load_dwordx4 v[90:93], v64, s[40:43], s52 offen nt\n\t"
      "buffer_load_dwordx4 v[94:97], v65, s[40:43], s52 offen nt\n\t"
      "s_add_u32 s52, s52, 4096\n\t"
      "s_bitset0_b32 s52, 15\n\t"
      "buffer_load_dwordx4 v[98:101], v62, s[40:43], s52 offen nt\n\t"
      "buffer_load_dwordx4 v[102:105], v63, s[40:43], s52 offen nt\n\t"
      "buffer_load_dwordx4 v[106:109], v64, s[40:43], s52 offen nt\n\t"
      "buffer_load_dwordx4 v[110:113], v65, s[40:43], s52 offen nt\n\t"
      "s_bitset1_b32 s52, 15\n\t"
      "buffer_load_dwordx4 v[114:117], v62, s[40:43], s52 offen nt\n\t"
      "buffer_load_dwordx4 v[118:121], v63, s[40:43], s52 offen nt\n\t"
      "buffer_load_dwordx4 v[122:125], v64, s[40:43], s52 offen nt\n\t"
      "buffer_load_dwordx4 v[126:129], v65, s[40:43], s52 offen nt\n\t"
      "s_add_u32 s52, s52, 4096\n\t"
      "s_bitset0_b32 s52, 15\n\t"
      "buffer_load_dwordx4 v[130:133], v62, s[40:43], s52 offen nt\n\t"
      "buffer_load_dwordx4 v[134:137], v63, s[40:43], s52 offen nt\n\t"
      "buffer_load_dwordx4 v[138:141], v64, s[40:43], s52 offen nt\n\t"
      "buffer_load_dwordx4 v[142:145], v65, s[40:43], s52 offen nt\n\t"
      "s_bitset1_b32 s52, 15\n\t"
      "buffer_load_dwordx4 v[146:149], v62, s[40:43], s52 offen nt\n\t"
      "buffer_load_dwordx4 v[150:153], v63, s[40:43], s52 offen nt\n\t"
      "buffer_load_dwordx4 v[154:157], v64, s[40:43], s52 offen nt\n\t"
      "buffer_load_dwordx4 v[158:161], v65, s[40:43], s52 offen nt\n\t"
      "s_add_u32 s52, s52, 4096\n\t"
      "s_bitset0_b32 s52, 15\n\t"
      "207:\n\t"
      "s_add_u32 s46, s72, s60\n\t"
      "s_addc_u32 s47, s73, 0\n\t"
      "s_and_b32 s47, s47, 0xffff\n\t"
      "v_and_b32 v11, 15, %[lane]\n\t"
      "v_lshlrev_b32 v2, 4, v11\n\t"
      "v_lshl_add_u32 v2, v53, 10, v2\n\t"
      "v_lshlrev_b32 v3, 9, %[lane]\n\t"
      "v_mov_b32 v5, 0xffff\n\t"
      "v_lshl_add_u32 v54, v11, 2, v53\n\t"
      "v_lshlrev_b32 v54, 4, v54\n\t"
      "v_add_u32 v55, 4096, v54\n\t"
      "v_and_b32 v56, 7, %[lane]\n\t"
      "v_lshlrev_b32 v56, 4, v56\n\t"
      "v_and_b32 v53, 1, v52\n\t"
      "v_lshlrev_b32 v53, 4, v53\n\t"
      "v_lshlrev_b32 v5, v53, v5\n\t"
      "v_lshrrev_b32 v53, 1, v52\n\t"
      "v_sub_u32 v52, 0, v53\n\t"
      "v_and_b32 v6, v5, v52\n\t"
      "v_xor_b32 v5, v5, v6\n\t"
      "v_and_b32 v58, 60, %[lane]\n\t"
      "v_mov_b32 v59, v58\n\t"
      "v_mov_b32 v36, 0\n\t"
      "v_mov_b32 v37, 0\n\t"
      "v_mov_b32 v38, 0\n\t"
      "v_mov_b32 v39, 0\n\t"
      "v_mov_b32 v40, 0\n\t"
      "v_mov_b32 v41, 0\n\t"
      "v_mov_b32 v42, 0\n\t"
      "v_mov_b32 v43, 0\n\t"
      "v_mov_b32 v46, 0\n\t"
      "v_mov_b32 v7, 0xff7fffff\n\t"
      "s_mov_b32 s53, 0\n\t"
      "s_mov_b32 s55, 0\n\t"
      "s_mov_b32 s82, 0\n\t"
      "s_mov_b64 vcc, 0\n\t"
      "s_add_u32 s38, s36, 230\n\t"
      "s_lshl_b32 s38, s38, 8\n\t"
      "s_movk_i32 s81, 20183\n\t"
      "s_movk_i32 s78, 250\n\t"
      "s_movk_i32 s79, 18\n\t"
      "s_movk_i32 s56, 18\n\t"
      "s_movk_i32 s54, 18\n\t"
      "s_mov_b32 s34, 0\n\t"
      "s_mov_b32 s44, 1073741824\n\t"
      "s_getpc_b64 s[88:89]\n\t"
      "200:\n\t"
      "s_add_u32 s84, s88, 210f-200b\n\t"
      "s_addc_u32 s85, s89, 0\n\t"
      "s_add_u32 s88, s88, 220f-200b\n\t"
      "s_addc_u32 s89, s89, 0\n\t"
      "s_lshl_b32 s59, s58, 13\n\t"
      "s_add_u32 s59, s59, 1048576\n\t"
      "s_add_u32 s68, s76, s59\n\t"
      "s_addc_u32 s69, s77, 0\n\t"
      "s_and_b32 s69, s69, 0xffff\n\t"
      "s_movk_i32 s70, 0x2000\n\t"
      "s_mov_b32 s71, 0x20000\n\t"
      "s_add_u32 s72, s74, s61\n\t"
      "s_addc_u32 s73, s75, 0\n\t"
      "s_and_b32 s73, s73, 0xffff\n\t"
      "s_movk_i32 s74, 0x100\n\t"
      "s_mov_b32 s75, 0x20000\n\t"
      "s_waitcnt vmcnt(20)\n\t"
      "v_readfirstlane_b32 s57, v14\n\t"
      "s_nop 0\n\t"
      "s_add_u32 s57, s57, 1\n\t"
      "v_mov_b32 v49, s57\n\t"
      "v_mov_b32 v51, s57\n\t"
      "v_mov_b32 v45, s57\n\t"
      "v_mov_b32 v47, s57\n\t"
      "s_branch 30f\n\t"
      "10:\n\t"
      "s_bitcmp1_b32 s53, 10\n\t"
      "s_cbranch_scc1 40f\n\t"
      "30:\n\t"
      "s_sub_u32 s54, s54, 1\n\t"
      "s_cbranch_scc1 20f\n\t"
      "50:\n\t"
      "s_lshl2_add_u32 s53, s53, s55\n\t"
      "s_cbranch_vccnz 150f\n\t"
      "s_waitcnt vmcnt(17)\n\t"
      "150:\n\t"
      "v_mfma_f32_16x16x32_bf16 v[16:19], v[20:23], v[66:69], 0\n\t"
      "s_waitcnt vmcnt(22)\n\t"
      "buffer_load_dwordx4 v[66:69], v62, s[40:43], s52 offen nt\n\t"
      "v_mfma_f32_16x16x32_bf16 v[16:19], v[24:27], v[70:73], v[16:19]\n\t"
      "s_waitcnt vmcnt(22)\n\t"
      "buffer_load_dwordx4 v[70:73], v63, s[40:43], s52 offen nt\n\t"
      "v_mfma_f32_16x16x32_bf16 v[16:19], v[28:31], v[74:77], v[16:19]\n\t"
      "s_waitcnt vmcnt(22)\n\t"
      "buffer_load_dwordx4 v[74:77], v64, s[40:43], s52 offen nt\n\t"
      "v_mfma_f32_16x16x32_bf16 v[16:19], v[32:35], v[78:81], v[16:19]\n\t"
      "s_waitcnt vmcnt(22)\n\t"
      "buffer_load_dwordx4 v[78:81], v65, s[40:43], s52 offen nt\n\t"
      "s_bitset1_b32 s52, 15\n\t"
      "s_nop 4\n\t"
      "v_mov_b32_dpp v10, v16 quad_perm:[0,0,0,0] row_mask:0xf bank_mask:0xf\n\t"
      "v_add_f32_dpp v10, v17, v10 quad_perm:[1,1,1,1] row_mask:0xf bank_mask:0xf\n\t"
      "v_add_f32_dpp v10, v18, v10 quad_perm:[2,2,2,2] row_mask:0xf bank_mask:0xf\n\t"
      "v_add_f32_dpp v10, v19, v10 quad_perm:[3,3,3,3] row_mask:0xf bank_mask:0xf\n\t"
      "ds_write_b32 v58, v10\n\t"
      "v_max_f32 v7, v7, v10\n\t"
      "v_add_u32 v58, 64, v58\n\t"
      "s_bitcmp1_b32 s53, 10\n\t"
      "s_cbranch_scc1 41f\n\t"
      "31:\n\t"
      "s_sub_u32 s54, s54, 1\n\t"
      "s_cbranch_scc1 21f\n\t"
      "51:\n\t"
      "s_lshl2_add_u32 s53, s53, s55\n\t"
      "s_cbranch_vccnz 151f\n\t"
      "s_waitcnt vmcnt(17)\n\t"
      "151:\n\t"
      "v_mfma_f32_16x16x32_bf16 v[16:19], v[20:23], v[82:85], 0\n\t"
      "s_waitcnt vmcnt(22)\n\t"
      "buffer_load_dwordx4 v[82:85], v62, s[40:43], s52 offen nt\n\t"
      "v_mfma_f32_16x16x32_bf16 v[16:19], v[24:27], v[86:89], v[16:19]\n\t"
      "s_waitcnt vmcnt(22)\n\t"
      "buffer_load_dwordx4 v[86:89], v63, s[40:43], s52 offen nt\n\t"
      "v_mfma_f32_16x16x32_bf16 v[16:19], v[28:31], v[90:93], v[16:19]\n\t"
      "s_waitcnt vmcnt(22)\n\t"
      "buffer_load_dwordx4 v[90:93], v64, s[40:43], s52 offen nt\n\t"
      "v_mfma_f32_16x16x32_bf16 v[16:19], v[32:35], v[94:97], v[16:19]\n\t"
      "s_waitcnt vmcnt(22)\n\t"
      "buffer_load_dwordx4 v[94:97], v65, s[40:43], s52 offen nt\n\t"
      "s_add_u32 s52, s52, 4096\n\t"
      "s_bitset0_b32 s52, 15\n\t"
      "s_nop 3\n\t"
      "v_mov_b32_dpp v10, v16 quad_perm:[0,0,0,0] row_mask:0xf bank_mask:0xf\n\t"
      "v_add_f32_dpp v10, v17, v10 quad_perm:[1,1,1,1] row_mask:0xf bank_mask:0xf\n\t"
      "v_add_f32_dpp v10, v18, v10 quad_perm:[2,2,2,2] row_mask:0xf bank_mask:0xf\n\t"
      "v_add_f32_dpp v10, v19, v10 quad_perm:[3,3,3,3] row_mask:0xf bank_mask:0xf\n\t"
      "ds_write_b32 v58, v10\n\t"
      "v_max_f32 v7, v7, v10\n\t"
      "v_add_u32 v58, 64, v58\n\t"
      "s_bitcmp1_b32 s53, 10\n\t"
      "s_cbranch_scc1 42f\n\t"
      "32:\n\t"
      "s_sub_u32 s54, s54, 1\n\t"
      "s_cbranch_scc1 22f\n\t"
      "52:\n\t"
      "s_lshl2_add_u32 s53, s53, s55\n\t"
      "s_cbranch_vccnz 152f\n\t"
      "s_waitcnt vmcnt(17)\n\t"
      "152:\n\t"
      "v_mfma_f32_16x16x32_bf16 v[16:19], v[20:23], v[98:101], 0\n\t"
      "s_waitcnt vmcnt(22)\n\t"
      "buffer_load_dwordx4 v[98:101], v62, s[40:43], s52 offen nt\n\t"
      "v_mfma_f32_16x16x32_bf16 v[16:19], v[24:27], v[102:105], v[16:19]\n\t"
      "s_waitcnt vmcnt(22)\n\t"
      "buffer_load_dwordx4 v[102:105], v63, s[40:43], s52 offen nt\n\t"
      "v_mfma_f32_16x16x32_bf16 v[16:19], v[28:31], v[106:109], v[16:19]\n\t"
      "s_waitcnt vmcnt(22)\n\t"
      "buffer_load_dwordx4 v[106:109], v64, s[40:43], s52 offen nt\n\t"
      "v_mfma_f32_16x16x32_bf16 v[16:19], v[32:35], v[110:113], v[16:19]\n\t"
      "s_waitcnt vmcnt(22)\n\t"
      "buffer_load_dwordx4 v[110:113], v65, s[40:43], s52 offen nt\n\t"
      "s_bitset1_b32 s52, 15\n\t"
      "s_nop 4\n\t"
      "v_mov_b32_dpp v10, v16 quad_perm:[0,0,0,0] row_mask:0xf bank_mask:0xf\n\t"
      "v_add_f32_dpp v10, v17, v10 quad_perm:[1,1,1,1] row_mask:0xf bank_mask:0xf\n\t"
      "v_add_f32_dpp v10, v18, v10 quad_perm:[2,2,2,2] row_mask:0xf bank_mask:0xf\n\t"
      "v_add_f32_dpp v10, v19, v10 quad_perm:[3,3,3,3] row_mask:0xf bank_mask:0xf\n\t"
      "ds_write_b32 v58, v10\n\t"
      "v_max_f32 v7, v7, v10\n\t"
      "v_add_u32 v58, 64, v58\n\t"
      "s_bitcmp1_b32 s53, 10\n\t"
      "s_cbranch_scc1 43f\n\t"
      "33:\n\t"
      "s_sub_u32 s54, s54, 1\n\t"
      "s_cbranch_scc1 23f\n\t"
      "53:\n\t"
      "s_lshl2_add_u32 s53, s53, s55\n\t"
      "s_cbranch_vccnz 153f\n\t"
      "s_waitcnt vmcnt(17)\n\t"
      "153:\n\t"
      "v_mfma_f32_16x16x32_bf16 v[16:19], v[20:23], v[114:117], 0\n\t"
      "s_waitcnt vmcnt(22)\n\t"
      "buffer_load_dwordx4 v[114:117], v62, s[40:43], s52 offen nt\n\t"
      "v_mfma_f32_16x16x32_bf16 v[16:19], v[24:27], v[118:121], v[16:19]\n\t"
      "s_waitcnt vmcnt(22)\n\t"
      "buffer_load_dwordx4 v[118:121], v63, s[40:43], s52 offen nt\n\t"
      "v_mfma_f32_16x16x32_bf16 v[16:19], v[28:31], v[122:125], v[16:19]\n\t"
      "s_waitcnt vmcnt(22)\n\t"
      "buffer_load_dwordx4 v[122:125], v64, s[40:43], s52 offen nt\n\t"
      "v_mfma_f32_16x16x32_bf16 v[16:19], v[32:35], v[126:129], v[16:19]\n\t"
      "s_waitcnt vmcnt(22)\n\t"
      "buffer_load_dwordx4 v[126:129], v65, s[40:43], s52 offen nt\n\t"
      "s_add_u32 s52, s52, 4096\n\t"
      "s_bitset0_b32 s52, 15\n\t"
      "s_nop 3\n\t"
      "v_mov_b32_dpp v10, v16 quad_perm:[0,0,0,0] row_mask:0xf bank_mask:0xf\n\t"
      "v_add_f32_dpp v10, v17, v10 quad_perm:[1,1,1,1] row_mask:0xf bank_mask:0xf\n\t"
      "v_add_f32_dpp v10, v18, v10 quad_perm:[2,2,2,2] row_mask:0xf bank_mask:0xf\n\t"
      "v_add_f32_dpp v10, v19, v10 quad_perm:[3,3,3,3] row_mask:0xf bank_mask:0xf\n\t"
      "ds_write_b32 v58, v10\n\t"
      "v_max_f32 v7, v7, v10\n\t"
      "v_add_u32 v58, 64, v58\n\t"
      "s_bitcmp1_b32 s53, 10\n\t"
      "s_cbranch_scc1 44f\n\t"
      "34:\n\t"
      "s_sub_u32 s54, s54, 1\n\t"
      "s_cbranch_scc1 24f\n\t"
      "54:\n\t"
      "s_lshl2_add_u32 s53, s53, s55\n\t"
      "s_cbranch_vccnz 154f\n\t"
      "s_waitcnt vmcnt(17)\n\t"
      "154:\n\t"
      "v_mfma_f32_16x16x32_bf16 v[16:19], v[20:23], v[130:133], 0\n\t"
      "s_waitcnt vmcnt(22)\n\t"
      "buffer_load_dwordx4 v[130:133], v62, s[40:43], s52 offen nt\n\t"
      "v_mfma_f32_16x16x32_bf16 v[16:19], v[24:27], v[134:137], v[16:19]\n\t"
      "s_waitcnt vmcnt(22)\n\t"
      "buffer_load_dwordx4 v[134:137], v63, s[40:43], s52 offen nt\n\t"
      "v_mfma_f32_16x16x32_bf16 v[16:19], v[28:31], v[138:141], v[16:19]\n\t"
      "s_waitcnt vmcnt(22)\n\t"
      "buffer_load_dwordx4 v[138:141], v64, s[40:43], s52 offen nt\n\t"
      "v_mfma_f32_16x16x32_bf16 v[16:19], v[32:35], v[142:145], v[16:19]\n\t"
      "s_waitcnt vmcnt(22)\n\t"
      "buffer_load_dwordx4 v[142:145], v65, s[40:43], s52 offen nt\n\t"
      "s_bitset1_b32 s52, 15\n\t"
      "s_nop 4\n\t"
      "v_mov_b32_dpp v10, v16 quad_perm:[0,0,0,0] row_mask:0xf bank_mask:0xf\n\t"
      "v_add_f32_dpp v10, v17, v10 quad_perm:[1,1,1,1] row_mask:0xf bank_mask:0xf\n\t"
      "v_add_f32_dpp v10, v18, v10 quad_perm:[2,2,2,2] row_mask:0xf bank_mask:0xf\n\t"
      "v_add_f32_dpp v10, v19, v10 quad_perm:[3,3,3,3] row_mask:0xf bank_mask:0xf\n\t"
      "ds_write_b32 v58, v10\n\t"
      "v_max_f32 v7, v7, v10\n\t"
      "v_add_u32 v58, 64, v58\n\t"
      "s_bitcmp1_b32 s53, 10\n\t"
      "s_cbranch_scc1 45f\n\t"
      "35:\n\t"
      "s_sub_u32 s54, s54, 1\n\t"
      "s_cbranch_scc1 25f\n\t"
      "55:\n\t"
      "s_lshl2_add_u32 s53, s53, s55\n\t"
      "s_cbranch_vccnz 155f\n\t"
      "s_waitcnt vmcnt(17)\n\t"
      "155:\n\t"
      "v_mfma_f32_16x16x32_bf16 v[16:19], v[20:23], v[146:149], 0\n\t"
      "s_waitcnt vmcnt(22)\n\t"
      "buffer_load_dwordx4 v[146:149], v62, s[40:43], s52 offen nt\n\t"
      "v_mfma_f32_16x16x32_bf16 v[16:19], v[24:27], v[150:153], v[16:19]\n\t"
      "s_waitcnt vmcnt(22)\n\t"
      "buffer_load_dwordx4 v[150:153], v63, s[40:43], s52 offen nt\n\t"
      "v_mfma_f32_16x16x32_bf16 v[16:19], v[28:31], v[154:157], v[16:19]\n\t"
      "s_waitcnt vmcnt(22)\n\t"
      "buffer_load_dwordx4 v[154:157], v64, s[40:43], s52 offen nt\n\t"
      "v_mfma_f32_16x16x32_bf16 v[16:19], v[32:35], v[158:161], v[16:19]\n\t"
      "s_waitcnt vmcnt(22)\n\t"
      "buffer_load_dwordx4 v[158:161], v65, s[40:43], s52 offen nt\n\t"
      "s_add_u32 s52, s52, 4096\n\t"
      "s_bitset0_b32 s52, 15\n\t"
      "s_waitcnt lgkmcnt(0)\n\t"
      "s_lshl_b32 s39, s36, 8\n\t"
      "s_sub_u32 s39, s39, s38\n\t"
      "s_sub_u32 s37, s39, s34\n\t"
      "s_cmp_gt_i32 s37, s44\n\t"
      "s_cselect_b32 s37, s37, 0\n\t"
      "s_add_u32 s38, s38, s37\n\t"
      "s_sub_u32 s39, s39, s37\n\t"
      "s_mov_b32 s34, s39\n\t"
      "s_add_u32 s38, s38, s81\n\t"
      "s_cmp_gt_i32 s39, 0\n\t"
      "s_cselect_b64 vcc, -1, 0\n\t"
      "s_memrealtime s[36:37]\n\t"
      "v_mov_b32_dpp v10, v16 quad_perm:[0,0,0,0] row_mask:0xf bank_mask:0xf\n\t"
      "v_add_f32_dpp v10, v17, v10 quad_perm:[1,1,1,1] row_mask:0xf bank_mask:0xf\n\t"
      "v_add_f32_dpp v10, v18, v10 quad_perm:[2,2,2,2] row_mask:0xf bank_mask:0xf\n\t"
      "v_add_f32_dpp v10, v19, v10 quad_perm:[3,3,3,3] row_mask:0xf bank_mask:0xf\n\t"
      "ds_write_b32 v58, v10\n\t"
      "v_max_f32 v7, v7, v10\n\t"
      "v_add_u32 v58, 64, v58\n\t"
      "s_branch 10b\n\t"
      "11:\n\t"
      "s_bitcmp1_b32 s53, 10\n\t"
      "s_cbranch_scc0 199f\n\t"
      "70:\n\t"
      "s_sub_u32 s54, s54, 1\n\t"
      "s_cbranch_scc1 60f\n\t"
      "90:\n\t"
      "s_waitcnt lgkmcnt(0)\n\t"
      "v_fmamk_f32 v11, v57, 0x3e0293ee, v15\n\t"
      "v_exp_f32 v11, v11\n\t"
      "v_add_u32 v59, 64, v59\n\t"
      "s_lshl2_add_u32 s53, s53, s55\n\t"
      "v_cvt_pk_bf16_f32 v14, v11, v11\n\t"
      "v_and_b32 v8, v5, v14\n\t"
      "v_and_b32 v9, v6, v14\n\t"
      "v_add_f32 v46, v46, v11\n\t"
      "ds_read_b32 v57, v59\n\t"
      "s_cbranch_vccnz 160f\n\t"
      "s_waitcnt vmcnt(17)\n\t"
      "160:\n\t"
      "v_mfma_f32_4x4x4_16b_bf16 v[36:39], v[8:9], v[66:67], v[36:39] cbsz:2 abid:0\n\t"
      "v_mfma_f32_4x4x4_16b_bf16 v[40:43], v[8:9], v[68:69], v[40:43] cbsz:2 abid:0\n\t"
      "s_waitcnt vmcnt(22)\n\t"
      "buffer_load_dwordx4 v[66:69], v62, s[40:43], s52 offen nt\n\t"
      "v_mfma_f32_4x4x4_16b_bf16 v[36:39], v[8:9], v[70:71], v[36:39] cbsz:2 abid:1\n\t"
      "v_mfma_f32_4x4x4_16b_bf16 v[40:43], v[8:9], v[72:73], v[40:43] cbsz:2 abid:1\n\t"
      "s_waitcnt vmcnt(22)\n\t"
      "buffer_load_dwordx4 v[70:73], v63, s[40:43], s52 offen nt\n\t"
      "v_mfma_f32_4x4x4_16b_bf16 v[36:39], v[8:9], v[74:75], v[36:39] cbsz:2 abid:2\n\t"
      "v_mfma_f32_4x4x4_16b_bf16 v[40:43], v[8:9], v[76:77], v[40:43] cbsz:2 abid:2\n\t"
      "s_waitcnt vmcnt(22)\n\t"
      "buffer_load_dwordx4 v[74:77], v64, s[40:43], s52 offen nt\n\t"
      "v_mfma_f32_4x4x4_16b_bf16 v[36:39], v[8:9], v[78:79], v[36:39] cbsz:2 abid:3\n\t"
      "v_mfma_f32_4x4x4_16b_bf16 v[40:43], v[8:9], v[80:81], v[40:43] cbsz:2 abid:3\n\t"
      "s_waitcnt vmcnt(22)\n\t"
      "buffer_load_dwordx4 v[78:81], v65, s[40:43], s52 offen nt\n\t"
      "s_bitset1_b32 s52, 15\n\t"
      "s_bitcmp1_b32 s53, 10\n\t"
      "s_cbranch_scc0 199f\n\t"
      "71:\n\t"
      "s_sub_u32 s54, s54, 1\n\t"
      "s_cbranch_scc1 61f\n\t"
      "91:\n\t"
      "s_waitcnt lgkmcnt(0)\n\t"
      "v_fmamk_f32 v11, v57, 0x3e0293ee, v15\n\t"
      "v_exp_f32 v11, v11\n\t"
      "v_add_u32 v59, 64, v59\n\t"
      "s_lshl2_add_u32 s53, s53, s55\n\t"
      "v_cvt_pk_bf16_f32 v14, v11, v11\n\t"
      "v_and_b32 v8, v5, v14\n\t"
      "v_and_b32 v9, v6, v14\n\t"
      "v_add_f32 v46, v46, v11\n\t"
      "ds_read_b32 v57, v59\n\t"
      "s_cbranch_vccnz 161f\n\t"
      "s_waitcnt vmcnt(17)\n\t"
      "161:\n\t"
      "v_mfma_f32_4x4x4_16b_bf16 v[36:39], v[8:9], v[82:83], v[36:39] cbsz:2 abid:0\n\t"
      "v_mfma_f32_4x4x4_16b_bf16 v[40:43], v[8:9], v[84:85], v[40:43] cbsz:2 abid:0\n\t"
      "s_waitcnt vmcnt(22)\n\t"
      "buffer_load_dwordx4 v[82:85], v62, s[40:43], s52 offen nt\n\t"
      "v_mfma_f32_4x4x4_16b_bf16 v[36:39], v[8:9], v[86:87], v[36:39] cbsz:2 abid:1\n\t"
      "v_mfma_f32_4x4x4_16b_bf16 v[40:43], v[8:9], v[88:89], v[40:43] cbsz:2 abid:1\n\t"
      "s_waitcnt vmcnt(22)\n\t"
      "buffer_load_dwordx4 v[86:89], v63, s[40:43], s52 offen nt\n\t"
      "v_mfma_f32_4x4x4_16b_bf16 v[36:39], v[8:9], v[90:91], v[36:39] cbsz:2 abid:2\n\t"
      "v_mfma_f32_4x4x4_16b_bf16 v[40:43], v[8:9], v[92:93], v[40:43] cbsz:2 abid:2\n\t"
      "s_waitcnt vmcnt(22)\n\t"
      "buffer_load_dwordx4 v[90:93], v64, s[40:43], s52 offen nt\n\t"
      "v_mfma_f32_4x4x4_16b_bf16 v[36:39], v[8:9], v[94:95], v[36:39] cbsz:2 abid:3\n\t"
      "v_mfma_f32_4x4x4_16b_bf16 v[40:43], v[8:9], v[96:97], v[40:43] cbsz:2 abid:3\n\t"
      "s_waitcnt vmcnt(22)\n\t"
      "buffer_load_dwordx4 v[94:97], v65, s[40:43], s52 offen nt\n\t"
      "s_add_u32 s52, s52, 4096\n\t"
      "s_bitset0_b32 s52, 15\n\t"
      "s_bitcmp1_b32 s53, 10\n\t"
      "s_cbranch_scc0 199f\n\t"
      "72:\n\t"
      "s_sub_u32 s54, s54, 1\n\t"
      "s_cbranch_scc1 62f\n\t"
      "92:\n\t"
      "s_waitcnt lgkmcnt(0)\n\t"
      "v_fmamk_f32 v11, v57, 0x3e0293ee, v15\n\t"
      "v_exp_f32 v11, v11\n\t"
      "v_add_u32 v59, 64, v59\n\t"
      "s_lshl2_add_u32 s53, s53, s55\n\t"
      "v_cvt_pk_bf16_f32 v14, v11, v11\n\t"
      "v_and_b32 v8, v5, v14\n\t"
      "v_and_b32 v9, v6, v14\n\t"
      "v_add_f32 v46, v46, v11\n\t"
      "ds_read_b32 v57, v59\n\t"
      "s_cbranch_vccnz 162f\n\t"
      "s_waitcnt vmcnt(17)\n\t"
      "162:\n\t"
      "v_mfma_f32_4x4x4_16b_bf16 v[36:39], v[8:9], v[98:99], v[36:39] cbsz:2 abid:0\n\t"
      "v_mfma_f32_4x4x4_16b_bf16 v[40:43], v[8:9], v[100:101], v[40:43] cbsz:2 abid:0\n\t"
      "s_waitcnt vmcnt(22)\n\t"
      "buffer_load_dwordx4 v[98:101], v62, s[40:43], s52 offen nt\n\t"
      "v_mfma_f32_4x4x4_16b_bf16 v[36:39], v[8:9], v[102:103], v[36:39] cbsz:2 abid:1\n\t"
      "v_mfma_f32_4x4x4_16b_bf16 v[40:43], v[8:9], v[104:105], v[40:43] cbsz:2 abid:1\n\t"
      "s_waitcnt vmcnt(22)\n\t"
      "buffer_load_dwordx4 v[102:105], v63, s[40:43], s52 offen nt\n\t"
      "v_mfma_f32_4x4x4_16b_bf16 v[36:39], v[8:9], v[106:107], v[36:39] cbsz:2 abid:2\n\t"
      "v_mfma_f32_4x4x4_16b_bf16 v[40:43], v[8:9], v[108:109], v[40:43] cbsz:2 abid:2\n\t"
      "s_waitcnt vmcnt(22)\n\t"
      "buffer_load_dwordx4 v[106:109], v64, s[40:43], s52 offen nt\n\t"
      "v_mfma_f32_4x4x4_16b_bf16 v[36:39], v[8:9], v[110:111], v[36:39] cbsz:2 abid:3\n\t"
      "v_mfma_f32_4x4x4_16b_bf16 v[40:43], v[8:9], v[112:113], v[40:43] cbsz:2 abid:3\n\t"
      "s_waitcnt vmcnt(22)\n\t"
      "buffer_load_dwordx4 v[110:113], v65, s[40:43], s52 offen nt\n\t"
      "s_bitset1_b32 s52, 15\n\t"
      "s_bitcmp1_b32 s53, 10\n\t"
      "s_cbranch_scc0 199f\n\t"
      "73:\n\t"
      "s_sub_u32 s54, s54, 1\n\t"
      "s_cbranch_scc1 63f\n\t"
      "93:\n\t"
      "s_waitcnt lgkmcnt(0)\n\t"
      "v_fmamk_f32 v11, v57, 0x3e0293ee, v15\n\t"
      "v_exp_f32 v11, v11\n\t"
      "v_add_u32 v59, 64, v59\n\t"
      "s_lshl2_add_u32 s53, s53, s55\n\t"
      "v_cvt_pk_bf16_f32 v14, v11, v11\n\t"
      "v_and_b32 v8, v5, v14\n\t"
      "v_and_b32 v9, v6, v14\n\t"
      "v_add_f32 v46, v46, v11\n\t"
      "ds_read_b32 v57, v59\n\t"
      "s_cbranch_vccnz 163f\n\t"
      "s_waitcnt vmcnt(17)\n\t"
      "163:\n\t"
      "v_mfma_f32_4x4x4_16b_bf16 v[36:39], v[8:9], v[114:115], v[36:39] cbsz:2 abid:0\n\t"
      "v_mfma_f32_4x4x4_16b_bf16 v[40:43], v[8:9], v[116:117], v[40:43] cbsz:2 abid:0\n\t"
      "s_waitcnt vmcnt(22)\n\t"
      "buffer_load_dwordx4 v[114:117], v62, s[40:43], s52 offen nt\n\t"
      "v_mfma_f32_4x4x4_16b_bf16 v[36:39], v[8:9], v[118:119], v[36:39] cbsz:2 abid:1\n\t"
      "v_mfma_f32_4x4x4_16b_bf16 v[40:43], v[8:9], v[120:121], v[40:43] cbsz:2 abid:1\n\t"
      "s_waitcnt vmcnt(22)\n\t"
      "buffer_load_dwordx4 v[118:121], v63, s[40:43], s52 offen nt\n\t"
      "v_mfma_f32_4x4x4_16b_bf16 v[36:39], v[8:9], v[122:123], v[36:39] cbsz:2 abid:2\n\t"
      "v_mfma_f32_4x4x4_16b_bf16 v[40:43], v[8:9], v[124:125], v[40:43] cbsz:2 abid:2\n\t"
      "s_waitcnt vmcnt(22)\n\t"
      "buffer_load_dwordx4 v[122:125], v64, s[40:43], s52 offen nt\n\t"
      "v_mfma_f32_4x4x4_16b_bf16 v[36:39], v[8:9], v[126:127], v[36:39] cbsz:2 abid:3\n\t"
      "v_mfma_f32_4x4x4_16b_bf16 v[40:43], v[8:9], v[128:129], v[40:43] cbsz:2 abid:3\n\t"
      "s_waitcnt vmcnt(22)\n\t"
      "buffer_load_dwordx4 v[126:129], v65, s[40:43], s52 offen nt\n\t"
      "s_add_u32 s52, s52, 4096\n\t"
      "s_bitset0_b32 s52, 15\n\t"
      "s_bitcmp1_b32 s53, 10\n\t"
      "s_cbranch_scc0 199f\n\t"
      "74:\n\t"
      "s_sub_u32 s54, s54, 1\n\t"
      "s_cbranch_scc1 64f\n\t"
      "94:\n\t"
      "s_waitcnt lgkmcnt(0)\n\t"
      "v_fmamk_f32 v11, v57, 0x3e0293ee, v15\n\t"
      "v_exp_f32 v11, v11\n\t"
      "v_add_u32 v59, 64, v59\n\t"
      "s_lshl2_add_u32 s53, s53, s55\n\t"
      "v_cvt_pk_bf16_f32 v14, v11, v11\n\t"
      "v_and_b32 v8, v5, v14\n\t"
      "v_and_b32 v9, v6, v14\n\t"
      "v_add_f32 v46, v46, v11\n\t"
      "ds_read_b32 v57, v59\n\t"
      "s_cbranch_vccnz 164f\n\t"
      "s_waitcnt vmcnt(17)\n\t"
      "164:\n\t"
      "v_mfma_f32_4x4x4_16b_bf16 v[36:39], v[8:9], v[130:131], v[36:39] cbsz:2 abid:0\n\t"
      "v_mfma_f32_4x4x4_16b_bf16 v[40:43], v[8:9], v[132:133], v[40:43] cbsz:2 abid:0\n\t"
      "s_waitcnt vmcnt(22)\n\t"
      "buffer_load_dwordx4 v[130:133], v62, s[40:43], s52 offen nt\n\t"
      "v_mfma_f32_4x4x4_16b_bf16 v[36:39], v[8:9], v[134:135], v[36:39] cbsz:2 abid:1\n\t"
      "v_mfma_f32_4x4x4_16b_bf16 v[40:43], v[8:9], v[136:137], v[40:43] cbsz:2 abid:1\n\t"
      "s_waitcnt vmcnt(22)\n\t"
      "buffer_load_dwordx4 v[134:137], v63, s[40:43], s52 offen nt\n\t"
      "v_mfma_f32_4x4x4_16b_bf16 v[36:39], v[8:9], v[138:139], v[36:39] cbsz:2 abid:2\n\t"
      "v_mfma_f32_4x4x4_16b_bf16 v[40:43], v[8:9], v[140:141], v[40:43] cbsz:2 abid:2\n\t"
      "s_waitcnt vmcnt(22)\n\t"
      "buffer_load_dwordx4 v[138:141], v64, s[40:43], s52 offen nt\n\t"
      "v_mfma_f32_4x4x4_16b_bf16 v[36:39], v[8:9], v[142:143], v[36:39] cbsz:2 abid:3\n\t"
      "v_mfma_f32_4x4x4_16b_bf16 v[40:43], v[8:9], v[144:145], v[40:43] cbsz:2 abid:3\n\t"
      "s_waitcnt vmcnt(22)\n\t"
      "buffer_load_dwordx4 v[142:145], v65, s[40:43], s52 offen nt\n\t"
      "s_bitset1_b32 s52, 15\n\t"
      "s_bitcmp1_b32 s53, 10\n\t"
      "s_cbranch_scc0 199f\n\t"
      "75:\n\t"
      "s_sub_u32 s54, s54, 1\n\t"
      "s_cbranch_scc1 65f\n\t"
      "95:\n\t"
      "s_waitcnt lgkmcnt(0)\n\t"
      "v_fmamk_f32 v11, v57, 0x3e0293ee, v15\n\t"
      "v_exp_f32 v11, v11\n\t"
      "v_add_u32 v59, 64, v59\n\t"
      "s_lshl2_add_u32 s53, s53, s55\n\t"
      "v_cvt_pk_bf16_f32 v14, v11, v11\n\t"
      "v_and_b32 v8, v5, v14\n\t"
      "v_and_b32 v9, v6, v14\n\t"
      "v_add_f32 v46, v46, v11\n\t"
      "ds_read_b32 v57, v59\n\t"
      "s_cbranch_vccnz 165f\n\t"
      "s_waitcnt vmcnt(17)\n\t"
      "165:\n\t"
      "v_mfma_f32_4x4x4_16b_bf16 v[36:39], v[8:9], v[146:147], v[36:39] cbsz:2 abid:0\n\t"
      "v_mfma_f32_4x4x4_16b_bf16 v[40:43], v[8:9], v[148:149], v[40:43] cbsz:2 abid:0\n\t"
      "s_waitcnt vmcnt(22)\n\t"
      "buffer_load_dwordx4 v[146:149], v62, s[40:43], s52 offen nt\n\t"
      "v_mfma_f32_4x4x4_16b_bf16 v[36:39], v[8:9], v[150:151], v[36:39] cbsz:2 abid:1\n\t"
      "v_mfma_f32_4x4x4_16b_bf16 v[40:43], v[8:9], v[152:153], v[40:43] cbsz:2 abid:1\n\t"
      "s_waitcnt vmcnt(22)\n\t"
      "buffer_load_dwordx4 v[150:153], v63, s[40:43], s52 offen nt\n\t"
      "v_mfma_f32_4x4x4_16b_bf16 v[36:39], v[8:9], v[154:155], v[36:39] cbsz:2 abid:2\n\t"
      "v_mfma_f32_4x4x4_16b_bf16 v[40:43], v[8:9], v[156:157], v[40:43] cbsz:2 abid:2\n\t"
      "s_waitcnt vmcnt(22)\n\t"
      "buffer_load_dwordx4 v[154:157], v64, s[40:43], s52 offen nt\n\t"
      "v_mfma_f32_4x4x4_16b_bf16 v[36:39], v[8:9], v[158:159], v[36:39] cbsz:2 abid:3\n\t"
      "v_mfma_f32_4x4x4_16b_bf16 v[40:43], v[8:9], v[160:161], v[40:43] cbsz:2 abid:3\n\t"
      "s_waitcnt vmcnt(22)\n\t"
      "buffer_load_dwordx4 v[158:161], v65, s[40:43], s52 offen nt\n\t"
      "s_add_u32 s52, s52, 4096\n\t"
      "s_bitset0_b32 s52, 15\n\t"
      "s_waitcnt lgkmcnt(0)\n\t"
      "s_lshl_b32 s39, s36, 8\n\t"
      "s_sub_u32 s39, s39, s38\n\t"
      "s_sub_u32 s37, s39, s34\n\t"
      "s_cmp_gt_i32 s37, s44\n\t"
      "s_cselect_b32 s37, s37, 0\n\t"
      "s_add_u32 s38, s38, s37\n\t"
      "s_sub_u32 s39, s39, s37\n\t"
      "s_mov_b32 s34, s39\n\t"
      "s_add_u32 s38, s38, s81\n\t"
      "s_cmp_gt_i32 s39, 0\n\t"
      "s_cselect_b64 vcc, -1, 0\n\t"
      "s_memrealtime s[36:37]\n\t"
      "s_branch 11b\n\t"
      "20:\n\t"
      "s_swappc_b64 s[86:87], s[88:89]\n\t"
      "s_branch 50b\n\t"
      "60:\n\t"
      "s_swappc_b64 s[86:87], s[88:89]\n\t"
      "s_branch 90b\n\t"
      "40:\n\t"
      "s_swappc_b64 s[86:87], s[84:85]\n\t"
      "s_branch 70b\n\t"
      "21:\n\t"
      "s_swappc_b64 s[86:87], s[88:89]\n\t"
      "s_branch 51b\n\t"
      "61:\n\t"
      "s_swappc_b64 s[86:87], s[88:89]\n\t"
      "s_branch 91b\n\t"
      "41:\n\t"
      "s_swappc_b64 s[86:87], s[84:85]\n\t"
      "s_branch 71b\n\t"
      "22:\n\t"
      "s_swappc_b64 s[86:87], s[88:89]\n\t"
      "s_branch 52b\n\t"
      "62:\n\t"
      "s_swappc_b64 s[86:87], s[88:89]\n\t"
      "s_branch 92b\n\t"
      "42:\n\t"
      "s_swappc_b64 s[86:87], s[84:85]\n\t"
      "s_branch 72b\n\t"
      "23:\n\t"
      "s_swappc_b64 s[86:87], s[88:89]\n\t"
      "s_branch 53b\n\t"
      "63:\n\t"
      "s_swappc_b64 s[86:87], s[88:89]\n\t"
      "s_branch 93b\n\t"
      "43:\n\t"
      "s_swappc_b64 s[86:87], s[84:85]\n\t"
      "s_branch 73b\n\t"
      "24:\n\t"
      "s_swappc_b64 s[86:87], s[88:89]\n\t"
      "s_branch 54b\n\t"
      "64:\n\t"
      "s_swappc_b64 s[86:87], s[88:89]\n\t"
      "s_branch 94b\n\t"
      "44:\n\t"
      "s_swappc_b64 s[86:87], s[84:85]\n\t"
      "s_branch 74b\n\t"
      "25:\n\t"
      "s_swappc_b64 s[86:87], s[88:89]\n\t"
      "s_branch 55b\n\t"
      "65:\n\t"
      "s_swappc_b64 s[86:87], s[88:89]\n\t"
      "s_branch 95b\n\t"
      "45:\n\t"
      "s_swappc_b64 s[86:87], s[84:85]\n\t"
      "s_branch 75b\n\t"
      "210:\n\t"
      "ds_read_b32 v57, v59\n\t"
      "v_max_f32_dpp v44, v7, v7 row_ror:4 row_mask:0xf bank_mask:0xf\n\t"
      "s_nop 1\n\t"
      "v_max_f32_dpp v44, v44, v44 row_ror:8 row_mask:0xf bank_mask:0xf\n\t"
      "v_mov_b32 v53, v44\n\t"
      "s_nop 1\n\t"
      "v_permlane16_swap_b32 v44, v53\n\t"
      "v_max_f32 v44, v44, v53\n\t"
      "v_mov_b32 v53, v44\n\t"
      "s_nop 1\n\t"
      "v_permlane32_swap_b32 v44, v53\n\t"
      "v_max_f32 v44, v44, v53\n\t"
      "v_mul_f32 v15, 0xbe0293ee, v44\n\t"
      "s_setpc_b64 s[86:87]\n\t"
      "220:\n\t"
      "s_add_u32 s82, s82, 1\n\t"
      "s_cmp_gt_u32 s82, 64\n\t"
      "s_cbranch_scc1 199f\n\t"
      "s_sub_u32 s78, s78, s56\n\t"
      "s_sub_u32 s79, s79, s56\n\t"
      "s_cmp_lg_u32 s78, 0\n\t"
      "s_cbranch_scc1 260f\n\t"
      "s_cmp_eq_u32 s55, 0\n\t"
      "s_cbranch_scc0 250f\n\t"
      "s_mov_b32 s55, 1\n\t"
      "s_mov_b64 s[40:41], s[46:47]\n\t"
      "v_mov_b32 v62, v2\n\t"
      "v_add_u32 v63, 256, v2\n\t"
      "v_add_u32 v64, 512, v2\n\t"
      "v_add_u32 v65, 768, v2\n\t"
      "s_mov_b32 s52, s80\n\t"
      "s_movk_i32 s78, 256\n\t"
      "s_branch 260f\n\t"
      "250:\n\t"
      "s_mov_b32 s55, 2\n\t"
      "s_mov_b32 s42, 0\n\t"
      "s_movk_i32 s78, 64\n\t"
      "260:\n\t"
      "s_cmp_lg_u32 s79, 0\n\t"
      "s_cbranch_scc1 261f\n\t"
      "s_add_u32 s58, s34, 33280\n\t"
      "s_max_i32 s58, s58, 0\n\t"
      "s_sub_u32 s59, s58, 6400\n\t"
      "s_mul_i32 s58, s58, 10\n\t"
      "s_lshr_b32 s58, s58, 4\n\t"
      "s_max_i32 s58, s58, s59\n\t"
      "s_add_u32 s38, s38, s58\n\t"
      "s_sub_u32 s34, s34, s58\n\t"
      "s_mov_b32 s44, 25600\n\t"
      "s_mov_b32 s79, 1073741824\n\t"
      "261:\n\t"
      "s_min_u32 s56, s78, s79\n\t"
      "s_sub_u32 s54, s56, 1\n\t"
      "s_min_u32 s54, s54, 511\n\t"
      "s_setpc_b64 s[86:87]\n\t"
      "199:\n\t"
      "v_add_f32_dpp v46, v46, v46 row_ror:4 row_mask:0xf bank_mask:0xf\n\t"
      "s_lshl_b32 s58, s63, 10\n\t"
      "s_lshl_b32 s59, s63, 4\n\t"
      "v_permlane32_swap_b32 v36, v40\n\t"
      "v_permlane32_swap_b32 v37, v41\n\t"
      "v_permlane32_swap_b32 v38, v42\n\t"
      "v_permlane32_swap_b32 v39, v43\n\t"
      "v_add_f32_dpp v46, v46, v46 row_ror:8 row_mask:0xf bank_mask:0xf\n\t"
      "v_add_f32 v36, v36, v40\n\t"
      "v_add_f32 v38, v38, v42\n\t"
      "v_add_f32 v37, v37, v41\n\t"
      "v_add_f32 v39, v39, v43\n\t"
      "v_mov_b32 v52, v46\n\t"
      "v_permlane16_swap_b32 v36, v38\n\t"
      "v_permlane16_swap_b32 v37, v39\n\t"
      "v_permlane32_swap_b32 v46, v52\n\t"
      "v_add_f32 v48, v36, v38\n\t"
      "v_add_f32 v50, v37, v39\n\t"
      "v_add_f32 v46, v46, v52\n\t"
      "s_cmp_eq_u32 s63, 0\n\t"
      "s_cbranch_scc1 100f\n\t"
      "buffer_store_dwordx4 v[48:51], v54, s[68:71], s58 offen sc1\n\t"
      "v_mov_b32 v52, v46\n\t"
      "s_nop 1\n\t"
      "v_permlane16_swap_b32 v46, v52\n\t"
      "v_add_f32 v46, v46, v52\n\t"
      "buffer_store_dwordx4 v[44:47], v3, s[64:67], s59 offen offset:256 sc1\n\t"
      "s_branch 109f\n\t"
      "100:\n\t"
      "buffer_load_dwordx4 v[94:97], v56, s[64:67], 0 offen offset:256 sc1\n\t"
      "buffer_load_dwordx4 v[66:69], v54, s[68:71], 0 offen offset:1024 sc1\n\t"
      "buffer_load_dwordx4 v[70:73], v54, s[68:71], 0 offen offset:2048 sc1\n\t"
      "buffer_load_dwordx4 v[74:77], v54, s[68:71], 0 offen offset:3072 sc1\n\t"
      "buffer_load_dwordx4 v[78:81], v55, s[68:71], 0 offen offset:0 sc1\n\t"
      "buffer_load_dwordx4 v[82:85], v55, s[68:71], 0 offen offset:1024 sc1\n\t"
      "buffer_load_dwordx4 v[86:89], v55, s[68:71], 0 offen offset:2048 sc1\n\t"
      "buffer_load_dwordx4 v[90:93], v55, s[68:71], 0 offen offset:3072 sc1\n\t"
      "v_mov_b32 v52, v46\n\t"
      "s_mov_b32 s60, 0x01010101\n\t"
      "s_mov_b32 s61, 0x01010101\n\t"
      "v_permlane16_swap_b32 v46, v52\n\t"
      "s_mov_b32 s55, 0\n\t"
      "v_add_f32 v46, v46, v52\n\t"
      "s_sleep 7\n\t"
      "buffer_load_dwordx4 v[126:129], v56, s[64:67], 0 offen offset:256 sc1\n\t"
      "buffer_load_dwordx4 v[98:101], v54, s[68:71], 0 offen offset:1024 sc1\n\t"
      "buffer_load_dwordx4 v[102:105], v54, s[68:71], 0 offen offset:2048 sc1\n\t"
      "buffer_load_dwordx4 v[106:109], v54, s[68:71], 0 offen offset:3072 sc1\n\t"
      "buffer_load_dwordx4 v[110:113], v55, s[68:71], 0 offen offset:0 sc1\n\t"
      "buffer_load_dwordx4 v[114:117], v55, s[68:71], 0 offen offset:1024 sc1\n\t"
      "buffer_load_dwordx4 v[118:121], v55, s[68:71], 0 offen offset:2048 sc1\n\t"
      "buffer_load_dwordx4 v[122:125], v55, s[68:71], 0 offen offset:3072 sc1\n\t"
      "101:\n\t"
      "s_waitcnt vmcnt(15)\n\t"
      "v_cndmask_b32 v94, v94, v44, s[60:61]\n\t"
      "v_cndmask_b32 v95, v95, v45, s[60:61]\n\t"
      "v_cndmask_b32 v96, v96, v46, s[60:61]\n\t"
      "v_cndmask_b32 v97, v97, v47, s[60:61]\n\t"
      "v_bitop3_b32 v17, v95, v97, s57 bitop3:0x7e\n\t"
      "v_max_f32_dpp v10, v94, v94 quad_perm:[1,0,3,2] row_mask:0xf bank_mask:0xf\n\t"
      "s_nop 1\n\t"
      "v_max_f32_dpp v10, v10, v10 quad_perm:[2,3,0,1] row_mask:0xf bank_mask:0xf\n\t"
      "s_nop 1\n\t"
      "v_max_f32_dpp v10, v10, v10 row_half_mirror row_mask:0xf bank_mask:0xf\n\t"
      "v_sub_f32 v16, v94, v10\n\t"
      "v_mul_f32 v16, 0x3e0293ee, v16\n\t"
      "v_exp_f32 v16, v16\n\t"
      "s_nop 0\n\t"
      "v_mul_f32 v10, v16, v96\n\t"
      "s_nop 1\n\t"
      "v_add_f32_dpp v10, v10, v10 quad_perm:[1,0,3,2] row_mask:0xf bank_mask:0xf\n\t"
      "s_nop 1\n\t"
      "v_add_f32_dpp v10, v10, v10 quad_perm:[2,3,0,1] row_mask:0xf bank_mask:0xf\n\t"
      "s_nop 1\n\t"
      "v_add_f32_dpp v10, v10, v10 row_half_mirror row_mask:0xf bank_mask:0xf\n\t"
      "v_rcp_f32 v10, v10\n\t"
      "s_nop 0\n\t"
      "v_mul_f32 v16, v16, v10\n\t"
      "s_nop 1\n\t"
      "v_mul_f32_dpp v18, v16, v48 row_newbcast:0 row_mask:0xf bank_mask:0xf\n\t"
      "v_mul_f32_dpp v19, v16, v50 row_newbcast:0 row_mask:0xf bank_mask:0xf\n\t"
      "s_waitcnt vmcnt(14)\n\t"
      "v_bitop3_b32 v11, v67, v69, s57 bitop3:0x7e\n\t"
      "v_fmac_f32_dpp v18, v16, v66 row_newbcast:1 row_mask:0xf bank_mask:0xf\n\t"
      "v_or_b32 v17, v17, v11\n\t"
      "v_fmac_f32_dpp v19, v16, v68 row_newbcast:1 row_mask:0xf bank_mask:0xf\n\t"
      "s_waitcnt vmcnt(13)\n\t"
      "v_bitop3_b32 v11, v71, v73, s57 bitop3:0x7e\n\t"
      "v_fmac_f32_dpp v18, v16, v70 row_newbcast:2 row_mask:0xf bank_mask:0xf\n\t"
      "v_or_b32 v17, v17, v11\n\t"
      "v_fmac_f32_dpp v19, v16, v72 row_newbcast:2 row_mask:0xf bank_mask:0xf\n\t"
      "s_waitcnt vmcnt(12)\n\t"
      "v_bitop3_b32 v11, v75, v77, s57 bitop3:0x7e\n\t"
      "v_fmac_f32_dpp v18, v16, v74 row_newbcast:3 row_mask:0xf bank_mask:0xf\n\t"
      "v_or_b32 v17, v17, v11\n\t"
      "v_fmac_f32_dpp v19, v16, v76 row_newbcast:3 row_mask:0xf bank_mask:0xf\n\t"
      "s_waitcnt vmcnt(11)\n\t"
      "v_bitop3_b32 v11, v79, v81, s57 bitop3:0x7e\n\t"
      "v_fmac_f32_dpp v18, v16, v78 row_newbcast:4 row_mask:0xf bank_mask:0xf\n\t"
      "v_or_b32 v17, v17, v11\n\t"
      "v_fmac_f32_dpp v19, v16, v80 row_newbcast:4 row_mask:0xf bank_mask:0xf\n\t"
      "s_waitcnt vmcnt(10)\n\t"
      "v_bitop3_b32 v11, v83, v85, s57 bitop3:0x7e\n\t"
      "v_fmac_f32_dpp v18, v16, v82 row_newbcast:5 row_mask:0xf bank_mask:0xf\n\t"
      "v_or_b32 v17, v17, v11\n\t"
      "v_fmac_f32_dpp v19, v16, v84 row_newbcast:5 row_mask:0xf bank_mask:0xf\n\t"
      "s_waitcnt vmcnt(9)\n\t"
      "v_bitop3_b32 v11, v87, v89, s57 bitop3:0x7e\n\t"
      "v_fmac_f32_dpp v18, v16, v86 row_newbcast:6 row_mask:0xf bank_mask:0xf\n\t"
      "v_or_b32 v17, v17, v11\n\t"
      "v_fmac_f32_dpp v19, v16, v88 row_newbcast:6 row_mask:0xf bank_mask:0xf\n\t"
      "s_waitcnt vmcnt(8)\n\t"
      "v_bitop3_b32 v11, v91, v93, s57 bitop3:0x7e\n\t"
      "v_fmac_f32_dpp v18, v16, v90 row_newbcast:7 row_mask:0xf bank_mask:0xf\n\t"
      "v_or_b32 v17, v17, v11\n\t"
      "v_fmac_f32_dpp v19, v16, v92 row_newbcast:7 row_mask:0xf bank_mask:0xf\n\t"
      "v_cmp_ne_u32 vcc, 0, v17\n\t"
      "v_cvt_pk_bf16_f32 v18, v18, v19\n\t"
      "s_cbranch_vccz 105f\n\t"
      "buffer_load_dwordx4 v[94:97], v56, s[64:67], 0 offen offset:256 sc1\n\t"
      "buffer_load_dwordx4 v[66:69], v54, s[68:71], 0 offen offset:1024 sc1\n\t"
      "buffer_load_dwordx4 v[70:73], v54, s[68:71], 0 offen offset:2048 sc1\n\t"
      "buffer_load_dwordx4 v[74:77], v54, s[68:71], 0 offen offset:3072 sc1\n\t"
      "buffer_load_dwordx4 v[78:81], v55, s[68:71], 0 offen offset:0 sc1\n\t"
      "buffer_load_dwordx4 v[82:85], v55, s[68:71], 0 offen offset:1024 sc1\n\t"
      "buffer_load_dwordx4 v[86:89], v55, s[68:71], 0 offen offset:2048 sc1\n\t"
      "buffer_load_dwordx4 v[90:93], v55, s[68:71], 0 offen offset:3072 sc1\n\t"
      "s_waitcnt vmcnt(15)\n\t"
      "v_cndmask_b32 v126, v126, v44, s[60:61]\n\t"
      "v_cndmask_b32 v127, v127, v45, s[60:61]\n\t"
      "v_cndmask_b32 v128, v128, v46, s[60:61]\n\t"
      "v_cndmask_b32 v129, v129, v47, s[60:61]\n\t"
      "v_bitop3_b32 v17, v127, v129, s57 bitop3:0x7e\n\t"
      "v_max_f32_dpp v10, v126, v126 quad_perm:[1,0,3,2] row_mask:0xf bank_mask:0xf\n\t"
      "s_nop 1\n\t"
      "v_max_f32_dpp v10, v10, v10 quad_perm:[2,3,0,1] row_mask:0xf bank_mask:0xf\n\t"
      "s_nop 1\n\t"
      "v_max_f32_dpp v10, v10, v10 row_half_mirror row_mask:0xf bank_mask:0xf\n\t"
      "v_sub_f32 v16, v126, v10\n\t"
      "v_mul_f32 v16, 0x3e0293ee, v16\n\t"
      "v_exp_f32 v16, v16\n\t"
      "s_nop 0\n\t"
      "v_mul_f32 v10, v16, v128\n\t"
      "s_nop 1\n\t"
      "v_add_f32_dpp v10, v10, v10 quad_perm:[1,0,3,2] row_mask:0xf bank_mask:0xf\n\t"
      "s_nop 1\n\t"
      "v_add_f32_dpp v10, v10, v10 quad_perm:[2,3,0,1] row_mask:0xf bank_mask:0xf\n\t"
      "s_nop 1\n\t"
      "v_add_f32_dpp v10, v10, v10 row_half_mirror row_mask:0xf bank_mask:0xf\n\t"
      "v_rcp_f32 v10, v10\n\t"
      "s_nop 0\n\t"
      "v_mul_f32 v16, v16, v10\n\t"
      "s_nop 1\n\t"
      "v_mul_f32_dpp v18, v16, v48 row_newbcast:0 row_mask:0xf bank_mask:0xf\n\t"
      "v_mul_f32_dpp v19, v16, v50 row_newbcast:0 row_mask:0xf bank_mask:0xf\n\t"
      "s_waitcnt vmcnt(14)\n\t"
      "v_bitop3_b32 v11, v99, v101, s57 bitop3:0x7e\n\t"
      "v_fmac_f32_dpp v18, v16, v98 row_newbcast:1 row_mask:0xf bank_mask:0xf\n\t"
      "v_or_b32 v17, v17, v11\n\t"
      "v_fmac_f32_dpp v19, v16, v100 row_newbcast:1 row_mask:0xf bank_mask:0xf\n\t"
      "s_waitcnt vmcnt(13)\n\t"
      "v_bitop3_b32 v11, v103, v105, s57 bitop3:0x7e\n\t"
      "v_fmac_f32_dpp v18, v16, v102 row_newbcast:2 row_mask:0xf bank_mask:0xf\n\t"
      "v_or_b32 v17, v17, v11\n\t"
      "v_fmac_f32_dpp v19, v16, v104 row_newbcast:2 row_mask:0xf bank_mask:0xf\n\t"
      "s_waitcnt vmcnt(12)\n\t"
      "v_bitop3_b32 v11, v107, v109, s57 bitop3:0x7e\n\t"
      "v_fmac_f32_dpp v18, v16, v106 row_newbcast:3 row_mask:0xf bank_mask:0xf\n\t"
      "v_or_b32 v17, v17, v11\n\t"
      "v_fmac_f32_dpp v19, v16, v108 row_newbcast:3 row_mask:0xf bank_mask:0xf\n\t"
      "s_waitcnt vmcnt(11)\n\t"
      "v_bitop3_b32 v11, v111, v113, s57 bitop3:0x7e\n\t"
      "v_fmac_f32_dpp v18, v16, v110 row_newbcast:4 row_mask:0xf bank_mask:0xf\n\t"
      "v_or_b32 v17, v17, v11\n\t"
      "v_fmac_f32_dpp v19, v16, v112 row_newbcast:4 row_mask:0xf bank_mask:0xf\n\t"
      "s_waitcnt vmcnt(10)\n\t"
      "v_bitop3_b32 v11, v115, v117, s57 bitop3:0x7e\n\t"
      "v_fmac_f32_dpp v18, v16, v114 row_newbcast:5 row_mask:0xf bank_mask:0xf\n\t"
      "v_or_b32 v17, v17, v11\n\t"
      "v_fmac_f32_dpp v19, v16, v116 row_newbcast:5 row_mask:0xf bank_mask:0xf\n\t"
      "s_waitcnt vmcnt(9)\n\t"
      "v_bitop3_b32 v11, v119, v121, s57 bitop3:0x7e\n\t"
      "v_fmac_f32_dpp v18, v16, v118 row_newbcast:6 row_mask:0xf bank_mask:0xf\n\t"
      "v_or_b32 v17, v17, v11\n\t"
      "v_fmac_f32_dpp v19, v16, v120 row_newbcast:6 row_mask:0xf bank_mask:0xf\n\t"
      "s_waitcnt vmcnt(8)\n\t"
      "v_bitop3_b32 v11, v123, v125, s57 bitop3:0x7e\n\t"
      "v_fmac_f32_dpp v18, v16, v122 row_newbcast:7 row_mask:0xf bank_mask:0xf\n\t"
      "v_or_b32 v17, v17, v11\n\t"
      "v_fmac_f32_dpp v19, v16, v124 row_newbcast:7 row_mask:0xf bank_mask:0xf\n\t"
      "v_cmp_ne_u32 vcc, 0, v17\n\t"
      "v_cvt_pk_bf16_f32 v18, v18, v19\n\t"
      "s_cbranch_vccz 105f\n\t"
      "buffer_load_dwordx4 v[126:129], v56, s[64:67], 0 offen offset:256 sc1\n\t"
      "buffer_load_dwordx4 v[98:101], v54, s[68:71], 0 offen offset:1024 sc1\n\t"
      "buffer_load_dwordx4 v[102:105], v54, s[68:71], 0 offen offset:2048 sc1\n\t"
      "buffer_load_dwordx4 v[106:109], v54, s[68:71], 0 offen offset:3072 sc1\n\t"
      "buffer_load_dwordx4 v[110:113], v55, s[68:71], 0 offen offset:0 sc1\n\t"
      "buffer_load_dwordx4 v[114:117], v55, s[68:71], 0 offen offset:1024 sc1\n\t"
      "buffer_load_dwordx4 v[118:121], v55, s[68:71], 0 offen offset:2048 sc1\n\t"
      "buffer_load_dwordx4 v[122:125], v55, s[68:71], 0 offen offset:3072 sc1\n\t"
      "s_add_u32 s55, s55, 1\n\t"
      "s_cmp_lt_u32 s55, 0x100000\n\t"
      "s_cbranch_scc1 101b\n\t"
      "105:\n\t"
      "v_lshrrev_b32 v54, 2, v54\n\t"
      "buffer_store_dword v18, v54, s[72:75], 0 offen\n\t"
      "buffer_store_dword v49, v3, s[64:67], 0 offen\n\t"
      "109:\n\t"
      :
      : [ka] "s"(ka), [bid] "s"(blockIdx.x), [lane] "v"(threadIdx.x)
      : "v1", "v2", "v3", "v4", "v5", "v6", "v7", "v8", "v9", "v10", "v11", "v12", "v13", "v14", "v15",
        "v16", "v17", "v18", "v19", "v20", "v21", "v22", "v23", "v24", "v25", "v26", "v27", "v28", "v29",
        "v30", "v31", "v32", "v33", "v34", "v35", "v36", "v37", "v38", "v39", "v40", "v41", "v42", "v43",
        "v44", "v45", "v46", "v47", "v48", "v49", "v50", "v51", "v52", "v53", "v54", "v55", "v56", "v57",
        "v58", "v59", "v60", "v61", "v62", "v63", "v64", "v65", "v66", "v67", "v68", "v69", "v70", "v71",
        "v72", "v73", "v74", "v75", "v76", "v77", "v78", "v79", "v80", "v81", "v82", "v83", "v84", "v85",
        "v86", "v87", "v88", "v89", "v90", "v91", "v92", "v93", "v94", "v95", "v96", "v97", "v98", "v99",
        "v100", "v101", "v102", "v103", "v104", "v105", "v106", "v107", "v108", "v109", "v110", "v111",
        "v112", "v113", "v114", "v115", "v116", "v117", "v118", "v119", "v120", "v121", "v122", "v123",
        "v124", "v125", "v126", "v127", "v128", "v129", "v130", "v131", "v132", "v133", "v134", "v135",
        "v136", "v137", "v138", "v139", "v140", "v141", "v142", "v143", "v144", "v145", "v146", "v147",
        "v148", "v149", "v150", "v151", "v152", "v153", "v154", "v155", "v156", "v157", "v158", "v159",
        "v160", "v161", "v162", "v163", "v164", "v165",
        "s33", "s34", "s35", "s36", "s37", "s38", "s39", "s40", "s41", "s42", "s43", "s44", "s45", "s46",
        "s47", "s48", "s49", "s50", "s51", "s52", "s53", "s54", "s55", "s56", "s57", "s58", "s59", "s60",
        "s61", "s62", "s63", "s64", "s65", "s66", "s67", "s68", "s69", "s70", "s71", "s72", "s73", "s74",
        "s75", "s76", "s77", "s78", "s79", "s80", "s81", "s82", "s83", "s84", "s85", "s86", "s87", "s88",
        "s89", "vcc", "scc", "memory");
}

extern "C" void kernel_launch(void* const* d_in, const int* in_sizes, int n_in, void* d_out, int out_size, void* d_ws,
                              size_t ws_size, hipStream_t stream) {
  attn_decode<<<256, 64, 16512, stream>>>(d_in[0], d_in[1], d_in[2], d_out, d_ws);
}
```
